```python
import torch

def _softmax(x, axis=-1):
    return torch.softmax(x, dim=axis)

def _gelu(x):
    return torch.nn.functional.gelu(x, approximate='tanh')

def _silu(x):
    return torch.nn.functional.silu(x)

def _sigmoid(x):
    return torch.sigmoid(x)

def _layernorm(x, axis=-1, eps=1e-05):
    m = x.mean(dim=axis, keepdim=True)
    v = x.var(dim=axis, keepdim=True, unbiased=False)
    return (x - m) / torch.sqrt(v + eps)

def _scatter_add(tgt, idx, src, axis=0):
    return tgt.clone().index_add_(axis, idx, src)

def _segment_sum(src, seg, n):
    out = torch.zeros((n,) + src.shape[1:], dtype=src.dtype)
    return out.index_add_(0, seg, src)

def _pad(x, pw):
    flat = tuple((v for pair in reversed(pw) for v in pair))
    return torch.nn.functional.pad(x, flat)
kernel_name = 'adaLN_cond__sliding_block_cached__swiglu_ffn__prompt'

def setup_inputs():

    _w = lambda a: (lambda t: t.to(torch.bfloat16) if t.is_floating_point() else t)(torch.as_tensor(a))

    def _A_setup():
        B, Sq, D = (1, 2048, 576)
        Hq, Hkv, Sk, Dh = (32, 16, 2048, 128)
        assert Hq % Hkv == 0, (Hq, Hkv)
        return {'x': _w((lambda *s: torch.randn(*s, dtype=torch.bfloat16) * 0.05)(B, Sq, D)), 'k_cache': _w((lambda *s: torch.randn(*s, dtype=torch.bfloat16) * 3.2)(B, Hkv, Sk, Dh)), 'v_cache': _w((lambda *s: torch.randn(*s, dtype=torch.bfloat16) * 0.4)(B, Hkv, Sk, Dh)), 'wq': _w((lambda *s: torch.randn(*s, dtype=torch.bfloat16) * 3.2)(D, Hq * Dh) * D ** (-0.5)), 'wo': _w((lambda *s: torch.randn(*s, dtype=torch.bfloat16) * 0.4)(Hq * Dh, D) * (Hq * Dh) ** (-0.5))}
    _A_in = _A_setup()

    def _B_setup():
        B, S, D, F = (1, 2048, 576, 4608)
        return {'x': _w((lambda *s: torch.randn(*s, dtype=torch.bfloat16) * 0.05)(B, S, D)), 'w_gate': _w((lambda *s: torch.randn(*s, dtype=torch.bfloat16) * 0.05)(D, F)), 'w_up': _w((lambda *s: torch.randn(*s, dtype=torch.bfloat16) * 0.05)(D, F)), 'w_down': _w((lambda *s: torch.randn(*s, dtype=torch.bfloat16) * 0.05)(F, D))}
    _B_in = _B_setup()
    return {'A_x': _A_in['x'], 'A_k_cache': _A_in['k_cache'], 'A_v_cache': _A_in['v_cache'], 'A_wq': _A_in['wq'], 'A_wo': _A_in['wo'], 'B_x': _B_in['x'], 'B_w_gate': _B_in['w_gate'], 'B_w_up': _B_in['w_up'], 'B_w_down': _B_in['w_down'], 'c': _w((lambda *s: torch.randn(*s, dtype=torch.bfloat16) * 0.1)(1, 576) * 8.0), 'w_mod': _w((lambda *s: torch.randn(*s, dtype=torch.bfloat16) * 0.1)(576, 6 * 576) * 576 ** (-0.5) * 8.0)}

def reference(A_x, A_k_cache, A_v_cache, A_wq, A_wo, B_x, B_w_gate, B_w_up, B_w_down, c, w_mod):

    def _A_ref(x, k_cache, v_cache, wq, wo):
        B, Sq, D = x.shape
        Hq, Dh = (32, 128)
        Hkv, Sk = (k_cache.shape[1], k_cache.shape[2])
        W = min(4096, max(256, Sk // 4))
        rep = Hq // Hkv
        q = (lambda x, *d: x.permute(*d))((x @ wq).reshape(B, Sq, Hq, Dh), 0, 2, 1, 3)
        k = (lambda x, n, axis: torch.repeat_interleave(x, n, dim=axis))(k_cache[:, :, Sk - W:, :], rep, axis=1)
        v = (lambda x, n, axis: torch.repeat_interleave(x, n, dim=axis))(v_cache[:, :, Sk - W:, :], rep, axis=1)
        scores = torch.einsum('bhqd,bhkd->bhqk', q, k) * Dh ** (-0.5)
        p = _softmax(scores, axis=-1)
        o = torch.einsum('bhqk,bhkd->bhqd', p, v)
        return (lambda x, *d: x.permute(*d))(o, 0, 2, 1, 3).reshape(B, Sq, Hq * Dh) @ wo

    def _B_ref(x, w_gate, w_up, w_down):
        g = _silu(x @ w_gate)
        u = x @ w_up
        return g * u @ w_down
    x = A_x
    mod = (_silu(c) @ w_mod).reshape(c.shape[0], 6, 1, -1)
    sa, ca, ga, sb, cb, gb = (mod[:, 0].clone(), mod[:, 1].clone(), mod[:, 2].clone(), mod[:, 3].clone(), mod[:, 4].clone(), mod[:, 5].clone())
    h = _layernorm(x, axis=-1) * (1 + ca) + sa
    x = x + ga * _A_ref(h, A_k_cache, A_v_cache, A_wq, A_wo)
    h = _layernorm(x, axis=-1) * (1 + cb) + sb
    x = x + gb * _B_ref(h, B_w_gate, B_w_up, B_w_down)
    return x
```

```cpp
#include <hip/hip_runtime.h>
#include <stdint.h>
#include <utility>

typedef unsigned short u16;
typedef unsigned long long u64;
typedef short    s16x4  __attribute__((ext_vector_type(4)));
typedef short    s16x8  __attribute__((ext_vector_type(8)));
typedef __bf16   bf16x8 __attribute__((ext_vector_type(8)));
typedef __bf16   bf16x2 __attribute__((ext_vector_type(2)));
typedef float    f32x16 __attribute__((ext_vector_type(16)));
typedef float    f32x4  __attribute__((ext_vector_type(4)));
typedef float    f32x2  __attribute__((ext_vector_type(2)));
typedef unsigned u32x4  __attribute__((ext_vector_type(4)));
typedef unsigned u32x2  __attribute__((ext_vector_type(2)));
#define LAS __attribute__((address_space(3)))
#define GAS __attribute__((address_space(1)))
#define DI __device__ __forceinline__
#define PIN() __builtin_amdgcn_sched_barrier(0)

constexpr int S = 2048, D = 576, HKV = 16, SK = 2048, DH = 128, W = 512, F = 4608, NQ = 4096;
constexpr unsigned FLB = S * D * 2;
constexpr unsigned OCT = 16 * 9216;
constexpr unsigned WS_FLAG = 0, WS_MOD = 131072, WS_H1 = WS_MOD + 16 * 16384, WS_H2 = WS_H1 + FLB, WS_PA = WS_H2 + FLB, WS_PF = WS_PA + 16 * FLB, WS_END = WS_PF + 16 * FLB;
constexpr unsigned FG_H1 = 0, FG_H2 = 2048, FG_PA = 4096, FG_PF = 6144;
constexpr unsigned OOB = 0x80000000u;
constexpr int SC1 = 16;
#define RD_AUX 16
#define REFC 96.f
#define MODBAR bar(); __builtin_amdgcn_s_sleep(20)
#define SLP 4
constexpr unsigned CODE_BYTES = 45056;
#define PRIO 1

extern "C" __device__ u64 dispatch_id_() __asm("llvm.amdgcn.dispatch.id");

DI float bf2f(u16 x) { return __uint_as_float(((unsigned)x) << 16); }
DI float bflo(unsigned u) { return __uint_as_float(u << 16); }
DI float bfhi(unsigned u) { return __uint_as_float(u & 0xffff0000u); }
DI unsigned pk2(float lo, float hi) { f32x2 v = {lo, hi}; return __builtin_bit_cast(unsigned, __builtin_convertvector(v, bf16x2)); }
DI float rbf(float f) { return bflo(pk2(f, f)); }
DI unsigned rfl(unsigned x) { return __builtin_amdgcn_readfirstlane(x); }
DI u32x4 mk_srd(const void* p, unsigned bytes) {
  const uint64_t a = (uint64_t)p; u32x4 r = {(unsigned)a, (unsigned)(a >> 32) & 0xffffu, bytes, 0x00020000u}; return r;
}
template <int IMM> DI void dma(unsigned ldsaddr, unsigned voff, const u32x4& srd, unsigned soff) {
  asm volatile("s_mov_b32 m0, %0\n\ts_nop 0\n\tbuffer_load_dwordx4 %1, %2, %3 offen offset:%4 lds"
               :: "s"(ldsaddr), "v"(voff), "s"(srd), "s"(soff), "i"(IMM));
}
template <int IMM> DI void dma_sc1(unsigned ldsaddr, unsigned voff, const u32x4& srd, unsigned soff) {
  asm volatile("s_mov_b32 m0, %0\n\ts_nop 0\n\tbuffer_load_dwordx4 %1, %2, %3 offen offset:%4 sc1 lds"
               :: "s"(ldsaddr), "v"(voff), "s"(srd), "s"(soff), "i"(IMM));
}
DI void dma_half(unsigned ldsaddr, unsigned voff, const u32x4& srd, unsigned soff) {
  asm volatile("s_mov_b32 m0, %0\n\ts_mov_b32 exec_hi, 0\n\tbuffer_load_dwordx4 %1, %2, %3 offen lds\n\ts_mov_b32 exec_hi, -1"
               :: "s"(ldsaddr), "v"(voff), "s"(srd), "s"(soff));
}
DI void touch(unsigned ldsaddr, unsigned voff, const u32x4& srd, unsigned soff) {
  asm volatile("s_mov_b32 m0, %0\n\ts_nop 0\n\tbuffer_load_dword %1, %2, %3 offen lds" :: "s"(ldsaddr), "v"(voff), "s"(srd), "s"(soff));
}
template <int I> struct IC { static constexpr int v = I; };
template <int... Is, class Fn> DI void sfor_(std::integer_sequence<int, Is...>, Fn&& f) { (f(IC<Is>{}), ...); }
template <int N, class Fn> DI void sfor(Fn&& f) { sfor_(std::make_integer_sequence<int, N>{}, f); }
#define SL(name) [&](auto ic_##name) __attribute__((always_inline)) { constexpr int name = decltype(ic_##name)::v;
template <int N> DI void wait_end() { PIN(); asm volatile("s_waitcnt vmcnt(%0) lgkmcnt(0)" :: "i"(N) : "memory"); PIN(); }
DI void bar() { PIN(); asm volatile("s_barrier" ::: "memory"); PIN(); }
template <int SLOT, int NSLOT> DI void prio() {
  if constexpr (SLOT == 0) __builtin_amdgcn_s_setprio(3);
  else if constexpr (SLOT == NSLOT / 4) __builtin_amdgcn_s_setprio(2);
  else if constexpr (SLOT == NSLOT / 2) __builtin_amdgcn_s_setprio(1);
  else if constexpr (SLOT == (3 * NSLOT) / 4) __builtin_amdgcn_s_setprio(0);
}
DI s16x4 rd_h(const LAS char* p, int o) { return __builtin_amdgcn_ds_read_tr16_b64_v4i16((LAS s16x4*)(p + o)); }
DI bf16x8 cat(const s16x4& a, const s16x4& b) { return __builtin_bit_cast(bf16x8, __builtin_shufflevector(a, b, 0, 1, 2, 3, 4, 5, 6, 7)); }
DI bf16x8 rd_tr(const LAS char* p, int o0, int o1) { return cat(rd_h(p, o0), rd_h(p, o1)); }
DI bf16x8 rd128(const LAS char* p, int o) { return *(const LAS bf16x8*)(p + o); }
#define MFMA32(a, b, c) __builtin_amdgcn_mfma_f32_32x32x16_bf16((a), (b), (c), 0, 0, 0)
#define MFMA16(a, b, c) __builtin_amdgcn_mfma_f32_16x16x32_bf16((a), (b), (c), 0, 0, 0)
DI u32x4 pack8s(const f32x16& x, int o, float sc) {
  u32x4 w = {pk2(x[o] * sc, x[o + 1] * sc), pk2(x[o + 2] * sc, x[o + 3] * sc), pk2(x[o + 4] * sc, x[o + 5] * sc), pk2(x[o + 6] * sc, x[o + 7] * sc)};
  return w;
}
DI u32x4 pack8u(const f32x16& x, int o) {
  u32x4 w = {pk2(x[o], x[o + 1]), pk2(x[o + 2], x[o + 3]), pk2(x[o + 4], x[o + 5]), pk2(x[o + 6], x[o + 7])};
  return w;
}
DI u32x4 pack44(const f32x4& a, const f32x4& b) { u32x4 w = {pk2(a[0], a[1]), pk2(a[2], a[3]), pk2(b[0], b[1]), pk2(b[2], b[3])}; return w; }
DI float sum32(float v) { auto r = __builtin_amdgcn_permlane32_swap(__float_as_uint(v), __float_as_uint(v), false, false); return __uint_as_float(r[0]) + __uint_as_float(r[1]); }
DI float sum16(float v) { auto r = __builtin_amdgcn_permlane16_swap(__float_as_uint(v), __float_as_uint(v), false, false); return __uint_as_float(r[0]) + __uint_as_float(r[1]); }
DI float max32(float v) { auto r = __builtin_amdgcn_permlane32_swap(__float_as_uint(v), __float_as_uint(v), false, false); return fmaxf(__uint_as_float(r[0]), __uint_as_float(r[1])); }
template <int N> DI float ror(float v) { return __uint_as_float(__builtin_amdgcn_update_dpp(0u, __float_as_uint(v), 0x120 + N, 0xf, 0xf, false)); }
DI float sum_m8(float v) { v += ror<8>(v); return sum32(sum16(v)); }
DI float sum_m4(float v) { v += ror<4>(v); return sum_m8(v); }
__host__ __device__ constexpr int rd_lo(int slot, int T) { return slot < T ? 2 * slot : slot + T; }

DI void flag_set(char* ws, unsigned off, u64 tag) { __hip_atomic_store((GAS u64*)(ws + off), tag, __ATOMIC_RELAXED, __HIP_MEMORY_SCOPE_AGENT); }
DI void flag_wait(char* ws, unsigned off, u64 tag) {
#pragma unroll 1
  for (int it = 0; it < (1 << 15); ++it) {
    const u64 v = __hip_atomic_load((GAS u64*)(ws + off), __ATOMIC_RELAXED, __HIP_MEMORY_SCOPE_AGENT);
    if (__builtin_amdgcn_ballot_w64(v != tag) == 0) break;
    __builtin_amdgcn_s_sleep(SLP);
  }
  asm volatile("" ::: "memory");
}

DI void ld16(u32x4& r, unsigned voff, const u32x4& srd, unsigned soff) { asm volatile("buffer_load_dwordx4 %0, %1, %2, %3 offen" : "=v"(r) : "v"(voff), "s"(srd), "s"(soff)); }
DI void ld4(unsigned& r, unsigned voff, const u32x4& srd, unsigned soff) { asm volatile("buffer_load_dword %0, %1, %2, %3 offen" : "=v"(r) : "v"(voff), "s"(srd), "s"(soff)); }
DI void ld16_sc1(u32x4& r, unsigned voff, const u32x4& srd, unsigned soff) { asm volatile("buffer_load_dwordx4 %0, %1, %2, %3 offen sc1" : "=v"(r) : "v"(voff), "s"(srd), "s"(soff)); }
DI void ld8_sc1(u32x2& r, unsigned voff, const u32x4& srd, unsigned soff) { asm volatile("buffer_load_dwordx2 %0, %1, %2, %3 offen sc1" : "=v"(r) : "v"(voff), "s"(srd), "s"(soff)); }

DI bool prog_ok(u64 v, u64 tag, unsigned need) { const u64 d = v - tag; return __builtin_amdgcn_ballot_w64(!(d >= need && d < 16)) == 0; }
DI void prog_wait(char* ws, unsigned off, u64 tag, unsigned need) {
#pragma unroll 1
  for (int it = 0; it < (1 << 15); ++it) {
    if (prog_ok(__hip_atomic_load((GAS u64*)(ws + off), __ATOMIC_RELAXED, __HIP_MEMORY_SCOPE_AGENT), tag, need)) break;
    __builtin_amdgcn_s_sleep(SLP);
  }
  asm volatile("" ::: "memory");
}

#define TS(i)

constexpr int KA_LO = 32768, KA_UP0 = 98304, KA_UP = 16384, KA_K0 = 147456, L_SCR = 155648, L_DUMMY = 163584;
constexpr int KF_LO = 36864, KF_L0 = 73728, KF_L1 = 110592, KF_L2 = 36864, KF_UP0 = 12288, KF_UP = 8192, L_X2 = 147456, L_RED = 0;
constexpr float QS = 0.08838834764831845f * 1.4426950408889634f;
constexpr float PTHR = 1.152921504606846976e18f;
DI float swiglu(float gt, float up) { return gt * __builtin_amdgcn_rcpf(1.f + __builtin_amdgcn_exp2f(-1.4426950408889634f * gt)) * up; }

struct GW { u32x4 sW; unsigned vW, oW, oWh; };
template <int P> DI void gw_dma(const GW& c, unsigned wv, unsigned lo, unsigned t) {
  const unsigned lw = lo + (wv >> 2) * 18432u;
  if constexpr (P < 4) dma<128 * P>(lw + (wv & 3) * 1024u + (4096 - 128) * P, c.vW, c.sW, c.oW + t * (32u * 9216u));
  else                 dma_half(lw + 16384u + (wv & 3) * 512u, c.vW, c.sW, c.oWh + t * (32u * 9216u));
}

__global__ __launch_bounds__(512, 2) void k_block(const u16* __restrict__ x, const u16* __restrict__ kc, const u16* __restrict__ vc,
                                                  const u16* __restrict__ wq, const u16* __restrict__ wo, const u16* __restrict__ wg,
                                                  const u16* __restrict__ wu, const u16* __restrict__ wd, const u16* __restrict__ cvec,
                                                  const u16* __restrict__ wm, u16* __restrict__ out, char* ws) {
  extern __shared__ __attribute__((aligned(16))) char smem[];
  TS(0);
  const LAS char* sm = (const LAS char*)smem;
  LAS char* smw = (LAS char*)smem;
  const unsigned ldsb = (unsigned)(size_t)sm;
  const int tid = threadIdx.x, lane = tid & 63;
  const unsigned wv = rfl(tid >> 6), w3 = wv & 3, w4 = wv >> 2;
  const unsigned bid = blockIdx.x, j = 2 * (bid & 7) + ((bid >> 3) & 1), ct = bid >> 4;
  {
    uint64_t pc; asm volatile("s_getpc_b64 %0" : "=s"(pc));
    touch(ldsb + L_DUMMY, lane < 2 ? lane * 128u : OOB, mk_srd((const void*)(pc & ~127ull), CODE_BYTES), ((bid >> 3) * 8u + wv) * 256u);
    PIN();
  }
  const unsigned hi = lane >> 5, g = lane >> 4, g1 = g & 1, q4 = (lane >> 2) & 3, p4 = lane & 3, r7 = (lane >> 2) & 7, t16 = lane & 15;
  const u64 tag = (dispatch_id_() * 0x9E3779B97F4A7C15ull) ^ (u64)__builtin_amdgcn_dispatch_ptr() ^ 0x5bd1e9955bd1e995ull;
  const unsigned fgb = WS_FLAG + ct * 8192u, tag32 = (unsigned)(tag >> 32) ^ (unsigned)tag;
  const u32x4 sWs = mk_srd(ws, WS_END);
  const __amdgpu_buffer_rsrc_t rWs = __builtin_amdgcn_make_buffer_rsrc((void*)ws, (short)0, (int)WS_END, 0x00020000);
  const unsigned vL = lane * 16u;

  const u32x4 sWq = mk_srd(wq, D * NQ * 2), sK = mk_srd(kc, HKV * SK * DH * 2);
  const unsigned vWq = r7 * 8192u + hi * 64u + (p4 ^ ((wv & 1) * 2u)) * 16u, oWq = wv * (8u * 8192u) + j * 512u;
  const unsigned vK = r7 * 256u + hi * 64u + ((p4 ^ (((wv & 1) << 1) | (r7 >> 2))) * 16u), oKV = (j * SK + (SK - W) + wv * 8u) * 256u;
  auto wq_dma = [&](auto p_, unsigned lo, int t) __attribute__((always_inline)) {
    constexpr int P = decltype(p_)::v; dma<128 * P>(lo + wv * 4096 + 896 * P, vWq, sWq, oWq + (unsigned)t * (64u * 8192u)); };
  const unsigned slice = ct * 16u + j;
  const bool hasm = slice < 108;
  f32x4 q[8][2]; f32x16 o[4];
  bf16x8 ca[4], bp[2];
  auto zero_init = [&]() __attribute__((always_inline)) {
    const bf16x8 z = {}; ca[0] = ca[1] = ca[2] = ca[3] = z; bp[0] = bp[1] = z;
#pragma unroll
    for (int i = 0; i < 8; ++i) { q[i][0] = (f32x4){}; q[i][1] = (f32x4){}; asm volatile("" : "+v"(q[i][0])); asm volatile("" : "+v"(q[i][1])); }
#pragma unroll
    for (int i = 0; i < 4; ++i) { o[i] = (f32x16){}; asm volatile("" : "+v"(o[i])); asm volatile("" : "+v"(ca[i])); }
    asm volatile("" : "+v"(bp[0])); asm volatile("" : "+v"(bp[1]));
  };
    const unsigned tk = lane & 7, grp = lane >> 3, tok0 = ct * 128u + j * 8u + tk;
    const bool urgent = slice < 36;
    const u32x4 sX0 = mk_srd(x, FLB), sM = mk_srd(wm, D * 6 * D * 2), sC = mk_srd(cvec, D * 2);
    LAS f32x2* red0 = (LAS f32x2*)(smw + L_SCR);
    LAS float* mrd = (LAS float*)(smw + L_SCR + 1024);
    u32x4 xv, x8, wr[5]; unsigned cr[5];
    auto mod_load = [&]() __attribute__((always_inline)) {
#pragma unroll
      for (int i = 0; i < 5; ++i) ld4(cr[i], (unsigned)(lane >> 3) * 4u, sC, (i * 8u + wv) * 32u);
      if (urgent) { MODBAR; }
#pragma unroll
      for (int i = 0; i < 5; ++i)
        ld16(wr[i], (unsigned)(lane >> 2) * 6912u + p4 * 16u, sM, (i * 8u + wv) * (16u * 6912u) + slice * 64u);
      };
    float sv[5];
    auto mod_silu = [&]() __attribute__((always_inline)) {
      asm volatile("" : "+v"(cr[0]), "+v"(cr[1]), "+v"(cr[2]), "+v"(cr[3]), "+v"(cr[4]));
#pragma unroll
      for (int i = 0; i < 5; ++i) { const float cv = (lane & 4) ? bfhi(cr[i]) : bflo(cr[i]);
        sv[i] = rbf(cv * __builtin_amdgcn_rcpf(1.f + __builtin_amdgcn_exp2f(-1.4426950408889634f * cv))); asm volatile("" : "+v"(sv[i])); } };
    auto mod_comp = [&]() __attribute__((always_inline)) {
      asm volatile("" : "+v"(wr[0]), "+v"(wr[1]), "+v"(wr[2]), "+v"(wr[3]), "+v"(wr[4]));
      float acc[8] = {};
#pragma unroll
      for (int i = 0; i < 5; ++i)
#pragma unroll
        for (int e = 0; e < 4; ++e) { acc[2 * e] += sv[i] * bflo(wr[i][e]); acc[2 * e + 1] += sv[i] * bfhi(wr[i][e]); }
#pragma unroll
      for (int e = 0; e < 4; ++e) { const auto r = __builtin_amdgcn_permlane32_swap(__float_as_uint(acc[e]), __float_as_uint(acc[4 + e]), false, false); acc[e] = __uint_as_float(r[0]) + __uint_as_float(r[1]); }
#pragma unroll
      for (int e = 0; e < 2; ++e) { const auto r = __builtin_amdgcn_permlane16_swap(__float_as_uint(acc[e]), __float_as_uint(acc[2 + e]), false, false); acc[e] = __uint_as_float(r[0]) + __uint_as_float(r[1]);
        acc[e] += ror<4>(acc[e]); acc[e] += ror<8>(acc[e]); }
      if (t16 < 4) *(LAS f32x2*)(mrd + wv * 32 + p4 * 8 + g * 2) = (f32x2){acc[0], acc[1]}; };
    auto mod_pub = [&]() __attribute__((always_inline)) {
      f32x2 a = {};
#pragma unroll
      for (int i = 0; i < 8; ++i) a += *(const LAS f32x2*)(mrd + i * 32 + t16 * 2);
      const unsigned rep = w3 * 4u + g;
      __builtin_amdgcn_raw_buffer_store_b64((u32x2){pk2(a[0], a[1]), tag32}, rWs, (urgent || rep == 0) ? rep * 16384u + t16 * 8u : OOB, WS_MOD + slice * 128u, SC1); };
    const unsigned vTm = (unsigned)(lane % 3) * 6912u + (unsigned)(lane / 3) * 1024u, oTm = ((bid >> 3) * 8u + wv) * (3u * 6912u) + (bid & 7) * 128u;
    touch(ldsb + L_DUMMY, lane < ((bid & 7) < 2 ? 9 : 6) ? vTm : OOB, sM, oTm);
    ld16(xv, tok0 * 1152u + grp * 16u, sX0, wv * 128u);
    ld16(x8, tok0 * 1152u + grp * 16u, sX0, wv == 0 ? 1024u : OOB);
    if (urgent) mod_load();
    PIN();
    zero_init();
    PIN();
    if (urgent) asm volatile("s_waitcnt vmcnt(5)" ::: "memory"); else asm volatile("s_waitcnt vmcnt(0)" ::: "memory");
    asm volatile("" : "+v"(xv), "+v"(x8));
    PIN();
    auto stat = [&](const u32x4& v, unsigned slab) __attribute__((always_inline)) {
      float s = 0.f, q = 0.f;
#pragma unroll
      for (int e = 0; e < 4; ++e) s += bflo(v[e]) + bfhi(v[e]);
      const float m = sum_m8(s) * (1.f / 64);
#pragma unroll
      for (int e = 0; e < 4; ++e) { const float a = bflo(v[e]) - m, b = bfhi(v[e]) - m; q += a * a + b * b; }
      q = sum_m8(q);
      if (lane < 8) red0[slab * 8 + lane] = (f32x2){m, q}; };
    stat(xv, wv);
    if (wv == 0) stat(x8, 8);
    if (urgent) { mod_silu(); PIN(); asm volatile("s_waitcnt vmcnt(0)" ::: "memory"); PIN(); TS(16); mod_comp(); }
    asm volatile("s_waitcnt lgkmcnt(0)" ::: "memory");
    bar();
    if (urgent && wv >= 4) mod_pub();
    PIN();
    touch(ldsb + L_DUMMY, lane < 20 ? (unsigned)(lane >> 2) * 8192u + (unsigned)(lane & 3) * 128u : OOB, sWq, (ct * 8u + wv) * (5u * 8192u) + j * 512u);
    touch(ldsb + L_DUMMY, (lane >= ((bid & 7) < 2 ? 9 : 6) && lane < ((bid & 7) < 6 ? 21 : 18)) ? vTm : OOB, sM, oTm);
    PIN();
    f32x2 st[9]; float mu = 0.f, q2 = 0.f;
#pragma unroll
    for (int i = 0; i < 9; ++i) { st[i] = red0[i * 8 + tk]; mu += st[i][0]; }
    mu *= (1.f / 9);
#pragma unroll
    for (int i = 0; i < 9; ++i) { const float d = st[i][0] - mu; q2 += st[i][1] + 64.f * d * d; }
    const float rs = __builtin_amdgcn_rsqf(q2 * (1.f / D) + 1e-5f);
#pragma unroll
    for (int e = 0; e < 4; ++e) { xv[e] = pk2((bflo(xv[e]) - mu) * rs, (bfhi(xv[e]) - mu) * rs); x8[e] = pk2((bflo(x8[e]) - mu) * rs, (bfhi(x8[e]) - mu) * rs); }
    asm volatile("" : "+v"(xv)); asm volatile("" : "+v"(x8));
    TS(17);
    u32x4 gr[8];
    const unsigned mo = WS_MOD + ct * 16384u + wv * 256u, mo8 = WS_MOD + ct * 16384u + (wv == 0 ? 2048u : wv * 256u);
#pragma unroll 1
    for (int it = 0; it < (1 << 15); ++it) {
      unsigned bad = 0;
#pragma unroll
      for (int e = 0; e < 4; ++e) gr[e] = __builtin_amdgcn_raw_buffer_load_b128(rWs, grp * 32u + (e & 1) * 16u, mo + (e >> 1) * 2304u, SC1);
      if (wv == 0) {
#pragma unroll
        for (int e = 0; e < 4; ++e) gr[4 + e] = __builtin_amdgcn_raw_buffer_load_b128(rWs, grp * 32u + (e & 1) * 16u, mo8 + (e >> 1) * 2304u, SC1);
#pragma unroll
        for (int e = 4; e < 8; ++e) bad |= (gr[e][1] ^ tag32) | (gr[e][3] ^ tag32);
      }
#pragma unroll
      for (int e = 0; e < 4; ++e) bad |= (gr[e][1] ^ tag32) | (gr[e][3] ^ tag32);
      if (__builtin_amdgcn_ballot_w64(bad != 0) == 0) break;
      __builtin_amdgcn_s_sleep(SLP);
      asm volatile("" ::: "memory");
    }
    TS(18);
    const unsigned ho0 = (tok0 >> 4) * 18432u + (grp >> 2) * 1024u + (grp & 3) * 256u + (tok0 & 15) * 16u;
    auto emit = [&](const u32x4& v, unsigned slab, int z) __attribute__((always_inline)) {
      const unsigned sa[4] = {gr[z][0], gr[z][2], gr[z + 1][0], gr[z + 1][2]}, cs[4] = {gr[z + 2][0], gr[z + 2][2], gr[z + 3][0], gr[z + 3][2]};
      u32x4 o;
#pragma unroll
      for (int e = 0; e < 4; ++e)
        o[e] = pk2(fmaf(bflo(v[e]), rbf(1.f + bflo(cs[e])), bflo(sa[e])) * QS, fmaf(bfhi(v[e]), rbf(1.f + bfhi(cs[e])), bfhi(sa[e])) * QS);
      __builtin_amdgcn_raw_buffer_store_b128(o, rWs, ho0, WS_H1 + slab * 2048u, SC1); };
    emit(xv, wv, 0);
    if (wv == 0) emit(x8, 8, 4);
    PIN();
    sfor<4>(SL(p) wq_dma(IC<p>{}, ldsb, 0); });
    asm volatile("s_waitcnt vmcnt(4)" ::: "memory");
    TS(19);
    bar();
    if (tid == 0) flag_set(ws, fgb + FG_H1 + j * 128u, tag);
    PIN();
    sfor<4>(SL(p) wq_dma(IC<p>{}, ldsb + KA_LO, 1); });
    dma<0>(ldsb + KA_K0 + wv * 1024, r7 * 256u + hi * 64u + ((p4 ^ ((wv & 2) | (r7 >> 2))) * 16u), sK, (j * SK + (SK - W) + (wv >> 1) * 8u) * 256u + (wv & 1) * 128u);
    if (hasm && !urgent) mod_load();
    PIN();
  TS(1);
  flag_wait(ws, fgb + FG_H1 + wv * 256u + (lane & 1) * 128u, tag);
  const unsigned oH1 = WS_H1 + (ct * 8u + wv) * 18432u;
  dma_sc1<0>(ldsb + KA_UP0 + wv * 2048, vL, sWs, oH1); dma_sc1<1024>(ldsb + KA_UP0 + wv * 2048, vL, sWs, oH1);
  dma_sc1<0>(ldsb + KA_UP0 + KA_UP + wv * 2048, vL, sWs, oH1 + 2048u); dma_sc1<1024>(ldsb + KA_UP0 + KA_UP + wv * 2048, vL, sWs, oH1 + 2048u);
  PIN();
  if (hasm && !urgent) { mod_silu(); mod_comp(); }
  const u32x4 sV = mk_srd(vc, HKV * SK * DH * 2);
  const unsigned vV = r7 * 256u + hi * 64u + p4 * 16u;
  auto kv_dma = [&](auto p_, unsigned lo, int t) __attribute__((always_inline)) {
    constexpr int P = decltype(p_)::v;
    if constexpr (P < 2) dma<128 * P>(lo + wv * 2048 + 896 * P, vK, sK, oKV + (unsigned)t * 16384u + 8192u);
    else                 dma<128 * (P - 2)>(lo + 16384 + wv * 2048 + 896 * (P - 2), vV, sV, oKV + (unsigned)t * 16384u); };
  int sidx = 0;
  unsigned lf = ldsb + 2 * KA_LO, uf = ldsb + KA_UP0 + 2 * KA_UP;
  {
    const LAS char* pE = sm + g * 4096u + w4 * 2048u + q4 * 64u + g1 * 32u + p4 * 8u;
    const LAS char* pO = sm + g * 4096u + w4 * 2048u + q4 * 64u + (g1 ^ 1u) * 32u + p4 * 8u;
    const LAS char* pB = sm + KA_UP0 + w3 * 4096u + lane * 16u;
    wait_end<2>();
    TS(2);
    auto qstage = [&](auto nk_, int nt) __attribute__((always_inline)) {
      constexpr int NK = decltype(nk_)::v;
      const int dl = sidx == 2 ? -2 * KA_LO : KA_LO, du = sidx == 2 ? -2 * KA_UP : KA_UP;
      s16x4 al[16], ah[16]; bf16x8 b[2][2];
      bar();
      sfor<32>(SL(i)
        prio<i, 32>();
        constexpr int m = (i >> 1) - 4, cb = i & 1;
        if constexpr (m < 0) q[8 + m][cb] = MFMA16(ca[4 + m], bp[cb], q[8 + m][cb]);
        else q[m & 7][cb] = MFMA16(cat(al[m], ah[m]), b[m >> 3][cb], q[m & 7][cb]);
        PIN();
        auto rdA = [&](auto k_, auto half_) __attribute__((always_inline)) {
          constexpr int k = decltype(k_)::v, hf = decltype(half_)::v, off = (k >> 3) * 16384 + ((k & 7) >> 1) * 512 + hf * 256;
          if constexpr (hf) ah[k] = rd_h((k & 1) ? pO : pE, off); else al[k] = rd_h((k & 1) ? pO : pE, off);
        };
        if constexpr (i == 0) { b[0][0] = rd128(pB, 0); rdA(IC<0>{}, IC<0>{}); }
        else if constexpr (i == 1) { rdA(IC<0>{}, IC<1>{}); b[0][1] = rd128(pB, 2048); }
        else if constexpr (i < 6) { rdA(IC<i - 1>{}, IC<0>{}); rdA(IC<i - 1>{}, IC<1>{}); }
        else if constexpr (i < 28) rdA(IC<(i >> 1) + 2>{}, IC<(i & 1)>{});
        if constexpr (i == 13) b[1][0] = rd128(pB, 1024);
        if constexpr (i == 15) b[1][1] = rd128(pB, 3072);
        if constexpr (NK == 0) {
          if constexpr (i == 6)  dma_sc1<0>(uf + wv * 2048, vL, sWs, oH1 + (unsigned)nt * 2048u);
          if constexpr (i == 9)  dma_sc1<1024>(uf + wv * 2048, vL, sWs, oH1 + (unsigned)nt * 2048u);
          if constexpr (i == 12 || i == 16 || i == 19 || i == 22) wq_dma(IC<(i - 11) / 3>{}, lf, nt);
        } else if constexpr (i == 6 || i == 10 || i == 14 || i == 18) kv_dma(IC<(i - 6) / 4>{}, lf, nt);
        if constexpr (i == 17) pB += du;
        if constexpr (i == 28) pE += dl;
        if constexpr (i == 29) pO += dl;
        PIN();
      });
#pragma unroll
      for (int i = 0; i < 4; ++i) ca[i] = cat(al[12 + i], ah[12 + i]);
      bp[0] = b[1][0]; bp[1] = b[1][1];
      lf = ldsb + sidx * KA_LO; uf = ldsb + KA_UP0 + sidx * KA_UP; sidx = sidx == 2 ? 0 : sidx + 1;
      wait_end<NK == 0 ? 6 : 4>();
    };
#pragma unroll 1
    for (int t = 0; t < 7; ++t) qstage(IC<0>{}, t + 2);
    if (hasm && !urgent && wv == 4) mod_pub();
#pragma unroll 1
    for (int t = 0; t < 2; ++t) qstage(IC<1>{}, t);
  }
  TS(3);
  const u32x4 sWo = mk_srd(wo, NQ * D * 2);
  const unsigned vWo = r7 * 1152u + hi * 64u + (p4 ^ ((r7 >> 2) * 2u)) * 16u, oWo = (j * 256u + wv * 32u) * 1152u;
  bf16x8 ob[4][9];
  {
    const unsigned r32 = lane & 31;
    const unsigned laneK0 = (r32 >> 3) * 2048u + (r32 & 7) * 64u + ((hi ^ ((r32 >> 2) & 3)) * 16u);
    const LAS char* pK0 = sm + laneK0; const LAS char* pK1 = sm + (laneK0 ^ 32u);
    const LAS char* pV = sm + 16384u + hi * 256u + q4 * 64u + g1 * 32u + p4 * 8u;
    bf16x8 kf[8];
#pragma unroll
    for (int s = 0; s < 8; ++s) kf[s] = rd128(((s & 1) ? pK1 : pK0) + KA_K0, (s >> 1) * 512);
#pragma unroll
    for (int i = 0; i < 4; ++i) { q[4 + i][0] = MFMA16(ca[i], bp[0], q[4 + i][0]); q[4 + i][1] = MFMA16(ca[i], bp[1], q[4 + i][1]); }
    bf16x8 qf[8];
    f32x16 X0 = {}, X1, negm;
#pragma unroll
    for (int s = 0; s < 8; ++s) {
      const auto e0 = __builtin_amdgcn_permlane16_swap(pk2(q[s][0][0], q[s][0][1]), pk2(q[s][1][0], q[s][1][1]), false, false);
      const auto e1 = __builtin_amdgcn_permlane16_swap(pk2(q[s][0][2], q[s][0][3]), pk2(q[s][1][2], q[s][1][3]), false, false);
      qf[s] = __builtin_bit_cast(bf16x8, (u32x4){e0[0], e1[0], e0[1], e1[1]});
      X0 = MFMA32(kf[s], qf[s], X0);
    }
    float ls = 0.f;
    bf16x8 pc;
    { const bf16x8 z = {}; pc = z; ca[0] = ca[1] = ca[2] = ca[3] = z; }
    const unsigned vT0 = lane < 9 ? lane * 128u : OOB;
    const unsigned vT1 = lane < 15 ? (unsigned)(lane / 5) * 9216u + (unsigned)(lane % 5) * 128u : 0x80000000u;
    {
      float mx = X0[0];
#pragma unroll
      for (int i = 1; i < 16; ++i) mx = fmaxf(mx, X0[i]);
      mx = max32(mx) + REFC;
#pragma unroll
      for (int i = 0; i < 16; ++i) { X0[i] -= mx; negm[i] = -mx; }
    }
    TS(4);
    PIN();
    auto astage = [&](auto kind_, int t) __attribute__((always_inline)) {
      constexpr int KIND = decltype(kind_)::v;
      bf16x8 a[32]; u32x4 pp[4]; float ex[16];
      const int dl = sidx == 2 ? -2 * KA_LO : KA_LO;
      bar();
      sfor<32>(SL(sl)
        prio<sl, 32>();
        constexpr int m = sl - 4;
        if constexpr (m < 0) o[sl] = MFMA32(ca[sl], pc, o[sl]);
        else {
          constexpr int h = m >> 4, u = m & 15;
          if constexpr (u < 8) {
            f32x16& Xn = h ? X0 : X1;
            if constexpr (KIND != 2 || h == 0) { if constexpr (u == 0) Xn = MFMA32(a[m], qf[u], negm); else Xn = MFMA32(a[m], qf[u], Xn); }
          } else {
            constexpr int ks = (u - 8) >> 2, db = u & 3;
            o[db] = MFMA32(a[m], __builtin_bit_cast(bf16x8, pp[2 * h + ks]), o[db]);
          }
        }
        PIN();
        sfor<2>(SL(ri)
          constexpr int r = rd_lo(sl, 2) + ri;
          if constexpr (r < rd_lo(sl + 1, 2) && r < 32) {
            constexpr int h = r >> 4, u = r & 15;
            if constexpr (u < 8) { if constexpr (KIND != 2 || h == 0) a[r] = rd128((u & 1) ? pK1 : pK0, h * 8192 + (u >> 1) * 512); }
            else { constexpr int ks = 2 * h + ((u - 8) >> 2), db = u & 3, o2 = (2 * ks * 4 + db) * 512; a[r] = rd_tr(pV, o2, o2 + 2048); }
          }
        });
        {
          constexpr int hb = sl >> 4, u = sl & 15;
          const f32x16& Xc = hb ? X1 : X0;
          if constexpr (u >= 1) ls += ex[u == 1 ? 0 : u];
          if constexpr (u == 1) ls += ex[1];
          if constexpr (u >= 1 && (u & 1)) pp[2 * hb + (u >> 3)][(u >> 1) & 3] = pk2(ex[u - 1], ex[u]);
          if constexpr (u == 0) ex[0] = __builtin_amdgcn_exp2f(Xc[0]);
          if constexpr (u < 15) ex[u + 1] = __builtin_amdgcn_exp2f(Xc[u + 1]);
        }
        if constexpr (sl == 2 || sl == 6 || sl == 10 || sl == 14) {
          constexpr int P = sl >> 2;
          if constexpr (KIND == 0) kv_dma(IC<P>{}, lf, t + 2); else dma<0>(lf + wv * 4096 + 1024 * P, vWo, sWo, oWo + P * 9216u + (KIND - 1) * 128u);
        }
        if constexpr (KIND == 0 && sl == 4) {
          const unsigned un = (ct * 8u + wv) * 2u + (unsigned)(t & 1);
          const u32x4 sT = t < 2 ? sWo : mk_srd(t < 4 ? wg : wu, D * F * 2);
          touch(ldsb + L_DUMMY, t < 2 ? vT0 : vT1, sT, t < 2 ? j * 294912u + un * 1152u : ((j * 576u) & ~127u) + un * (3u * 9216u));
        }
        if constexpr (sl == 30) { pK0 += dl; pK1 += dl; }
        if constexpr (sl == 31) pV += dl;
        PIN();
      });
#pragma unroll
      for (int i = 0; i < 4; ++i) ca[i] = a[28 + i];
      pc = __builtin_bit_cast(bf16x8, pp[3]);
      lf = ldsb + sidx * KA_LO; sidx = sidx == 2 ? 0 : sidx + 1;
      if constexpr (KIND == 0) wait_end<5>(); else if constexpr (KIND == 1) wait_end<4>();
    };
#pragma unroll 1
    for (int t = 0; t < 6; ++t) astage(IC<0>{}, t);
    astage(IC<1>{}, 6);
    astage(IC<2>{}, 7);
#pragma unroll
    for (int i = 0; i < 4; ++i) o[i] = MFMA32(ca[i], pc, o[i]);
    TS(5);
    float lt = sum32(ls);
    if (__builtin_expect(__builtin_amdgcn_ballot_w64(!(lt <= PTHR)) != 0, 0)) {
      const u16* kb = kc + (size_t)(j * SK + (SK - W) + r32) * DH + hi * 8;
      const u16* vb = vc + (size_t)(j * SK + (SK - W) + hi * 4) * DH + r32;
      float mx = -__builtin_inff();
#pragma unroll 1
      for (int b = 0; b < 16; ++b) {
        f32x16 X = {};
#pragma unroll
        for (int s = 0; s < 8; ++s) X = MFMA32(*(const bf16x8*)(kb + b * 32 * DH + s * 16), qf[s], X);
#pragma unroll
        for (int i = 0; i < 16; ++i) mx = fmaxf(mx, X[i]);
      }
      mx = max32(mx);
      float l2 = 0.f;
#pragma unroll
      for (int d = 0; d < 4; ++d) o[d] = (f32x16){};
#pragma unroll 1
      for (int b = 0; b < 16; ++b) {
        f32x16 X = {};
#pragma unroll
        for (int s = 0; s < 8; ++s) X = MFMA32(*(const bf16x8*)(kb + b * 32 * DH + s * 16), qf[s], X);
#pragma unroll
        for (int i = 0; i < 16; ++i) { X[i] = __builtin_amdgcn_exp2f(X[i] - mx); l2 += X[i]; }
#pragma unroll
        for (int ks = 0; ks < 2; ++ks) {
          const bf16x8 pf = __builtin_bit_cast(bf16x8, pack8u(X, 8 * ks));
#pragma unroll
          for (int d = 0; d < 4; ++d) {
            s16x8 vf;
#pragma unroll
            for (int e = 0; e < 8; ++e) vf[e] = (short)vb[(b * 32 + ks * 16 + 8 * (e >> 2) + (e & 3)) * DH + d * 32];
            o[d] = MFMA32(__builtin_bit_cast(bf16x8, vf), pf, o[d]);
          }
        }
      }
      lt = sum32(l2);
    }
    const float rl = 1.f / lt;
    LAS char* xw = smw + KA_UP0 + (2 * w3 + g1) * 8192u + w4 * 4096u + hi * 256u + t16 * 16u;
#pragma unroll
    for (int s = 0; s < 8; ++s) *(LAS u32x4*)(xw + (s >> 1) * 1024 + (s & 1) * 512) = pack8s(o[s >> 1], (s & 1) * 8, rl);
    wait_end<4>();
  }
  TS(6);
  GW gw;
  gw.sW = mk_srd(w4 ? wu : wg, D * F * 2);
  gw.vW = (unsigned)(lane >> 2) * 9216u + ((p4 ^ (g1 * 2u)) * 16u);
  gw.oW = (wv & 1) * (16u * 9216u) + j * 576u + (w3 >> 1) * 64u; gw.oWh = w3 * (8u * 9216u) + j * 576u + 512u;
  const unsigned pvo = (t16 >> 2) * OCT + g * 64u + (t16 & 3) * 16u;
  const unsigned pbase = (ct * 16u + w4 * 8u) * OCT + j * 9216u + w3 * 256u;
  f32x4 accA[4], accB[4];
  u32x2 prx[2][2]; u32x4 prm[2][3];
  const unsigned vm = (unsigned)(lane >> 2) * 16u;
  {
    const u32x4 sX = mk_srd(x, FLB);
    const unsigned tA = ((ct * 16u + j) >> 2) * 32u + (j & 3) * 4u + p4, vx = tA * 1152u + (unsigned)(lane >> 2) * 8u;
#pragma unroll
    for (int z = 0; z < 2; ++z) {
      const unsigned so = z ? 0u : (wv + 1u) * 128u;
      ld8_sc1(prx[z][0], vx, sX, so); ld8_sc1(prx[z][1], vx, sX, so + 16u * 1152u);
#pragma unroll
      for (int e = 0; e < 3; ++e) ld16_sc1(prm[z][e], vm, sWs, WS_MOD + (2u + e) * 2304u + so * 2u);
    }
  }
  auto gran4 = [&](u32x4 r, unsigned vec, unsigned slab) __attribute__((always_inline)) {
#pragma unroll 1
    for (int it = 0; it < (1 << 15) && __builtin_amdgcn_ballot_w64(((r[1] ^ tag32) | (r[3] ^ tag32)) != 0) != 0; ++it) {
      r = __builtin_amdgcn_raw_buffer_load_b128(rWs, vm, WS_MOD + vec * 2304u + slab * 256u, SC1); asm volatile("" ::: "memory"); }
    return (u32x2){r[0], r[2]}; };
  auto unp4 = [&](const u32x2& r) __attribute__((always_inline)) { return (f32x4){bflo(r[0]), bfhi(r[0]), bflo(r[1]), bfhi(r[1])}; };
  u32x2 pm[2][3];
  const unsigned vF = tid == 0 ? 0u : OOB, vP = (wv * 2u + (lane & 1)) * 128u, ro = (ct * 16u + j) * OCT + wv * (2u * 9216u);
  u32x2 pvA = {}, pvB = {};
  auto side = [&](auto i_, auto poll_, u32x2& pv, int t, unsigned fP, unsigned wsP, unsigned lp) __attribute__((always_inline)) {
    constexpr int i = decltype(i_)::v;
    if constexpr (i == 28 && decltype(poll_)::v) ld8_sc1(pv, vP, sWs, t >= 3 ? fgb + fP : OOB);
    if constexpr (i == 2) { const u64 c = tag + (unsigned)(t > 2 ? t - 2 : 0); __builtin_amdgcn_raw_buffer_store_b64((u32x2){(unsigned)c, (unsigned)(c >> 32)}, rWs, vF, fgb + fP + j * 128u, SC1); }
    if constexpr (i == 3)  dma_sc1<0>(lp + wv * 2048u, vL, sWs, t >= 5 ? wsP + ro + (unsigned)(t - 5) * 1024u : OOB);
    if constexpr (i == 7)  dma_sc1<0>(lp + wv * 2048u + 1024u, vL, sWs, t >= 5 ? wsP + ro + 9216u + (unsigned)(t - 5) * 1024u : OOB);
  };
  auto side_end = [&](u32x2& pv, int t, unsigned fP) __attribute__((always_inline)) {
    asm volatile("" : "+v"(pv));
    if (t >= 4 && __builtin_expect(!prog_ok(((u64)pv[1] << 32) | pv[0], tag, t - 3), 0)) prog_wait(ws, fgb + fP + vP, tag, t - 3);
  };
  {
    const LAS char* pW = sm + 2 * KA_LO + (g >> 1) * 2048u + (w3 >> 1) * 512u + g1 * 256u + q4 * 64u + (((w3 & 1) ^ g1) * 32u) + p4 * 8u;
    const LAS char* const xr = sm + KA_UP0 + w4 * 32768u + lane * 16u;
    auto ostage = [&](auto mode_, int t, f32x4 (&acc)[4], f32x4 (&accn)[4], u32x2& pvo_, u32x2& pvn) __attribute__((always_inline)) {
      constexpr int MODE = decltype(mode_)::v;
      const unsigned so = WS_PA + pbase + (unsigned)(t - 1) * 1024u;
      const int dl = sidx == 2 ? -2 * KA_LO : KA_LO;
      bf16x8 a[8]; const f32x4 zero = {};
      bar();
      sfor<32>(SL(i)
        prio<i, 32>();
        constexpr int m = (i >> 2) - 2, cb = i & 3;
        if constexpr (m < 0) { if constexpr (MODE != 0) acc[cb] = MFMA16(ca[2 + m], ob[cb][8 + m], acc[cb]); }
        else if constexpr (m == 0) accn[cb] = MFMA16(a[m], ob[cb][m], zero);
        else accn[cb] = MFMA16(a[m], ob[cb][m], accn[cb]);
        PIN();
        constexpr int k = i < 24 ? ((i & 3) == 0 ? i >> 2 : -1) : (i == 24 ? 6 : (i == 26 ? 7 : -1));
        if constexpr (k >= 0) a[k] = rd_tr(pW, k * 4096, k * 4096 + 1024);
        if constexpr (MODE == 0) { if constexpr (i < 24) ob[i & 3][i >> 2] = rd128(xr, (i & 3) * 8192 + (i >> 2) * 1024);
                                   else if constexpr (i < 28) { ob[i & 3][6] = rd128(xr, (i & 3) * 8192 + 6144); ob[i & 3][7] = rd128(xr, (i & 3) * 8192 + 7168); } }
        constexpr int P = i == 1 ? 0 : (i == 5 ? 1 : (i == 9 ? 2 : (i == 17 ? 3 : (i == 25 ? 4 : -1))));
        if constexpr (MODE < 2 && P >= 0 && P < 4) dma<0>(lf + wv * 4096u + 1024u * P, vWo, sWo, oWo + P * 9216u + (unsigned)(t + 2) * 128u);
        if constexpr (MODE != 0) side(IC<i>{}, IC<MODE == 1>{}, pvn, t, FG_PA, WS_PA, ldsb + KA_UP0 + (unsigned)(t >= 5 ? t - 5 : 3) * 16384u);
        if constexpr (i == 13 && MODE != 0) __builtin_amdgcn_raw_buffer_store_b128(pack44(acc[0], acc[1]), rWs, pvo, so, SC1);
        if constexpr (i == 21 && MODE != 0) __builtin_amdgcn_raw_buffer_store_b128(pack44(acc[2], acc[3]), rWs, pvo, so + 4u * OCT, SC1);
        if constexpr (i == 29) pW += dl;
        PIN();
      });
      ca[0] = a[6]; ca[1] = a[7];
      lf = ldsb + sidx * KA_LO; sidx = sidx == 2 ? 0 : sidx + 1;
      wait_end<MODE == 0 ? 4 : (MODE == 1 ? 10 : 5)>();
      if constexpr (MODE != 0) { if (t < 8) side_end(pvo_, t, FG_PA); }
    };
    ostage(IC<0>{}, 0, accB, accA, pvB, pvA);
#pragma unroll 1
    for (int t = 1; t < 7; t += 2) { ostage(IC<1>{}, t, accA, accB, pvA, pvB); ostage(IC<1>{}, t + 1, accB, accA, pvB, pvA); }
    asm volatile("" : "+v"(prx[0][0]), "+v"(prx[0][1]), "+v"(prm[0][0]), "+v"(prm[0][1]), "+v"(prm[0][2]), "+v"(prx[1][0]), "+v"(prx[1][1]), "+v"(prm[1][0]), "+v"(prm[1][1]), "+v"(prm[1][2]));
#pragma unroll
    for (int z = 0; z < 2; ++z)
#pragma unroll
      for (int e = 0; e < 3; ++e) { pm[z][e] = gran4(prm[z][e], 2 + e, z ? 0u : wv + 1u); asm volatile("" : "+v"(pm[z][e])); }
    ostage(IC<2>{}, 7, accA, accB, pvA, pvB);
    ostage(IC<2>{}, 8, accB, accA, pvB, pvA);
#pragma unroll
    for (int i = 0; i < 2; ++i)
#pragma unroll
      for (int cb = 0; cb < 4; ++cb) accA[cb] = MFMA16(ca[i], ob[cb][6 + i], accA[cb]);
  }
  TS(7);
  auto seam = [&](auto tsb_, unsigned wsP, unsigned fP, unsigned l01, unsigned l23, auto&& post) __attribute__((always_inline)) {
    const unsigned so = wsP + pbase + 8u * 1024u, rb = wsP + (ct * 16u + j) * OCT;
    u32x4 p[16];
    auto adds = [&](float (&r)[8]) __attribute__((always_inline)) {
#pragma unroll
      for (int e = 0; e < 4; ++e) { r[2 * e] = bflo(p[0][e]); r[2 * e + 1] = bfhi(p[0][e]); }
#pragma unroll
      for (int i = 1; i < 16; ++i)
#pragma unroll
        for (int e = 0; e < 4; ++e) { r[2 * e] += bflo(p[i][e]); r[2 * e + 1] += bfhi(p[i][e]); } };
    __builtin_amdgcn_raw_buffer_store_b128(pack44(accA[0], accA[1]), rWs, pvo, so, SC1);
    __builtin_amdgcn_raw_buffer_store_b128(pack44(accA[2], accA[3]), rWs, pvo, so + 4u * OCT, SC1);
    asm volatile("s_waitcnt vmcnt(0)" ::: "memory");
    bar();
    TS(decltype(tsb_)::v);
    if (tid == 0) flag_set(ws, fgb + fP + j * 128u, tag + 9u);
    float r[8];
    if (wv < 3) {
      if (wv == 0) {
#pragma unroll
        for (int i = 0; i < 16; ++i) p[i] = *(const LAS u32x4*)(sm + l01 + i * 1024 + lane * 16u);
        adds(r); post(r, IC<1>{});
      }
      const unsigned lb = wv == 0 ? l01 + 16384u : l23 + (wv - 1u) * 16384u;
#pragma unroll
      for (int i = 0; i < 16; ++i) p[i] = *(const LAS u32x4*)(sm + lb + i * 1024 + lane * 16u);
    } else {
      prog_wait(ws, fgb + fP + t16 * 128u, tag, wv < 5 ? 6u : 9u);
      TS(decltype(tsb_)::v + 1);
#pragma unroll
      for (int i = 0; i < 16; ++i) p[i] = __builtin_amdgcn_raw_buffer_load_b128(rWs, vL, rb + (wv + 1u) * 1024u + i * 9216u, SC1);
    }
    PIN();
    adds(r); post(r, IC<0>{});
  };
  const unsigned tokA = ((ct * 16u + j) >> 2) * 32u + (j & 3) * 4u + p4;
  {
    LAS f32x2* red = (LAS f32x2*)(smw + L_RED);
    float v[8], v8[8];
    seam(IC<20>{}, WS_PA, FG_PA, KA_UP0, KA_UP0 + 32768, [&](float (&r)[8], auto z_) __attribute__((always_inline)) {
      constexpr int Z = decltype(z_)::v; const unsigned slab = Z ? 0u : wv + 1u;
      const unsigned xr[4] = {prx[Z][0][0], prx[Z][0][1], prx[Z][1][0], prx[Z][1][1]}; const f32x4 gv = unp4(pm[Z][0]);
      float (&d)[8] = Z ? v8 : v;
#pragma unroll
      for (int e = 0; e < 8; ++e) d[e] = rbf(((e & 1) ? bfhi(xr[e >> 1]) : bflo(xr[e >> 1])) + rbf(gv[e & 3] * rbf(r[e])));
      const float ma = sum_m4((d[0] + d[1]) + (d[2] + d[3])) * (1.f / 64), mb = sum_m4((d[4] + d[5]) + (d[6] + d[7])) * (1.f / 64);
      float qa = 0.f, qb = 0.f;
#pragma unroll
      for (int e = 0; e < 4; ++e) { const float da = d[e] - ma, db = d[4 + e] - mb; qa += da * da; qb += db * db; }
      qa = sum_m4(qa); qb = sum_m4(qb);
      if (lane < 4) { red[slab * 8 + lane] = (f32x2){ma, qa}; red[slab * 8 + 4 + lane] = (f32x2){mb, qb}; } });
    TS(8);
    asm volatile("s_waitcnt lgkmcnt(0)" ::: "memory");
    bar();
    f32x2 sa[9], sbb[9]; float ma = 0.f, mb = 0.f, qa = 0.f, qb = 0.f;
#pragma unroll
    for (int i = 0; i < 9; ++i) { sa[i] = red[i * 8 + p4]; sbb[i] = red[i * 8 + 4 + p4]; ma += sa[i][0]; mb += sbb[i][0]; }
    ma *= (1.f / 9); mb *= (1.f / 9);
#pragma unroll
    for (int i = 0; i < 9; ++i) { const float da = sa[i][0] - ma, db = sbb[i][0] - mb; qa += sa[i][1] + 64.f * da * da; qb += sbb[i][1] + 64.f * db * db; }
    const float ra = __builtin_amdgcn_rsqf(qa * (1.f / D) + 1e-5f), rb = __builtin_amdgcn_rsqf(qb * (1.f / D) + 1e-5f);
    const unsigned tok = tokA + g1 * 16u;
    const unsigned ho = (tok >> 4) * 18432u + hi * 1024u + q4 * 256u + (tok & 15) * 16u;
    auto part2 = [&](const float (&r)[8], const f32x4& sv, const f32x4& cv, unsigned slab) __attribute__((always_inline)) {
      float h[8];
#pragma unroll
      for (int e = 0; e < 8; ++e) h[e] = fmaf(rbf((r[e] - (e < 4 ? ma : mb)) * (e < 4 ? ra : rb)), rbf(1.f + cv[e & 3]), sv[e & 3]);
      const auto e0 = __builtin_amdgcn_permlane16_swap(pk2(h[0], h[1]), pk2(h[4], h[5]), false, false);
      const auto e1 = __builtin_amdgcn_permlane16_swap(pk2(h[2], h[3]), pk2(h[6], h[7]), false, false);
      __builtin_amdgcn_raw_buffer_store_b128((u32x4){e0[0], e1[0], e0[1], e1[1]}, rWs, ho, WS_H2 + slab * 2048u, SC1);
      *(LAS u32x4*)(smw + L_X2 + slab * 1024u + lane * 16u) = (u32x4){pk2(r[0], r[1]), pk2(r[2], r[3]), pk2(r[4], r[5]), pk2(r[6], r[7])}; };
    part2(v, unp4(pm[0][1]), unp4(pm[0][2]), wv + 1u);
    if (wv == 0) part2(v8, unp4(pm[1][1]), unp4(pm[1][2]), 0u);
    PIN();
  }
  const u32x4 sD = mk_srd(wd, F * D * 2);
  const unsigned oD = (j * 288u + wv * 8u) * 1152u, oDh = (j * 288u + 256u + (wv >> 1) * 8u) * 1152u + (wv & 1) * 64u;
  auto wd_dma = [&](auto p_, unsigned lo, unsigned toff) __attribute__((always_inline)) {
    constexpr int P = decltype(p_)::v;
    if constexpr (P < 4) dma<0>(lo + wv * 1024u + 8192u * P, vWo, sD, oD + (unsigned)P * (64u * 1152u) + toff);
    else                 dma_half(lo + 32768u + wv * 512u, vWo, sD, oDh + toff); };
  {
    f32x4 acc[10][4];
    bf16x8 bA[4], bB[4];
    PIN();
    sfor<5>(SL(p) gw_dma<p>(gw, wv, ldsb + KF_L0, 0); });
    asm volatile("s_waitcnt vmcnt(5)" ::: "memory");
    bar();
    if (tid == 0) flag_set(ws, fgb + FG_H2 + j * 128u, tag);
    PIN();
    sfor<5>(SL(p) gw_dma<p>(gw, wv, ldsb + KF_L1, 1); });
    TS(9);
    flag_wait(ws, fgb + FG_H2 + (wv >> 1) * 512u + p4 * 128u, tag);
    const unsigned oH2 = WS_H2 + (ct * 8u + wv) * 18432u;
    dma_sc1<0>(ldsb + KF_UP0 + wv * 1024u, vL, sWs, oH2);
    dma_sc1<0>(ldsb + KF_UP0 + KF_UP + wv * 1024u, vL, sWs, oH2 + 1024u);
    PIN();
    {
      const bf16x8 z = {};
#pragma unroll
      for (int i = 0; i < 10; ++i)
#pragma unroll
        for (int cb = 0; cb < 4; ++cb) if (i < 8 || cb < 2) { acc[i][cb] = (f32x4){}; asm volatile("" : "+v"(acc[i][cb])); }
#pragma unroll
      for (int i = 0; i < 3; ++i) { ca[i] = z; asm volatile("" : "+v"(ca[i])); }
#pragma unroll
      for (int i = 0; i < 4; ++i) { bB[i] = z; asm volatile("" : "+v"(bB[i])); }
    }
    sidx = 0; lf = ldsb + KF_L2; uf = ldsb + KF_UP0 + 2 * KF_UP;
    const unsigned lanec = KF_L0 + (g >> 1) * 512u + g1 * 256u + q4 * 64u + p4 * 8u;
    const LAS char* pE = sm + lanec + w3 * 4096u + g1 * 32u; const LAS char* pO = sm + lanec + w3 * 4096u + (g1 ^ 1u) * 32u;
    const LAS char* pX = sm + lanec + 16384u + (((w3 >> 1) ^ g1) * 32u);
    const LAS char* pB0 = sm + KF_UP0 + (w4 * 4u + (w3 & 1) * 2u) * 1024u + lane * 16u;
    const LAS char* pB1 = sm + KF_UP0 + (w4 * 4u + 2u - (w3 & 1) * 2u) * 1024u + lane * 16u;
    const unsigned vT = lane < 3 ? lane * 128u : 0x80000000u;
    wait_end<1>();
    TS(10);
    auto gstage = [&](auto nk_, auto nw_, auto tch_, unsigned toff, bf16x8 (&bq)[4], bf16x8 (&b)[4]) __attribute__((always_inline)) {
      constexpr int NK = decltype(nk_)::v, NW = decltype(nw_)::v; constexpr bool TCH = decltype(tch_)::v;
      const int dl = sidx == 1 ? -2 * KF_LO : KF_LO, du = sidx == 2 ? -2 * KF_UP : KF_UP;
      s16x4 al[10], ah[10];
      bar();
      sfor<36>(SL(i)
        prio<i, 36>();
        if constexpr (i < 4) acc[7][i] = MFMA16(ca[0], bq[i], acc[7][i]);
        else if constexpr (i < 8) acc[8 + ((i - 4) >> 1)][i & 1] = MFMA16(ca[1 + ((i - 4) >> 1)], bq[i & 1], acc[8 + ((i - 4) >> 1)][i & 1]);
        else { constexpr int m = (i - 8) >> 2, cb = i & 3; acc[m][cb] = MFMA16(cat(al[m], ah[m]), b[cb], acc[m][cb]); }
        PIN();
        auto rdA = [&](auto r_, auto half_) __attribute__((always_inline)) {
          constexpr int r = decltype(r_)::v, hf = decltype(half_)::v, fb = r >> 1, off = (r & 1) * 18432 + (fb < 4 ? (fb >> 1) * 2048 : 0) + hf * 1024;
          const LAS char* p = fb == 4 ? pX : ((fb & 1) ? pO : pE);
          if constexpr (hf) ah[r] = rd_h(p, off); else al[r] = rd_h(p, off);
        };
        if constexpr (i == 0) { b[0] = rd128(pB0, 0); rdA(IC<0>{}, IC<0>{}); }
        if constexpr (i == 1) { rdA(IC<0>{}, IC<1>{}); b[1] = rd128(pB0, 1024); }
        if constexpr (i == 2) { b[2] = rd128(pB1, 0); b[3] = rd128(pB1, 1024); }
        constexpr int k = i == 3 ? 1 : (i >= 8 && i <= 24 && (i & 3) == 0 ? i >> 2 : (i == 26 ? 7 : (i == 28 ? 8 : (i == 30 ? 9 : -1))));
        if constexpr (k >= 0) { rdA(IC<k>{}, IC<0>{}); rdA(IC<k>{}, IC<1>{}); }
        if constexpr (NK == 0 && i == 4) dma_sc1<0>(uf + wv * 1024u, vL, sWs, oH2 + toff * 1024u);
        if constexpr (i == 6 || i == 10 || i == 14 || i == 18 || i == 22) {
          if constexpr (NK == 0) gw_dma<(i - 6) / 4>(gw, wv, lf, toff); else wd_dma(IC<(i - 6) / 4>{}, lf, toff); }
        if constexpr (TCH && i == 5) touch(ldsb + L_DUMMY, vT, sD, j * 331776u + ((ct * 8u + wv) * 8u + (toff >> 1) - 1u) * 384u);
        if constexpr (i == 5)  { pB0 += du; pB1 += du; }
        if constexpr (i == 27) { pE += dl; }
        if constexpr (i == 29) { pO += dl; }
        if constexpr (i == 32) { pX += dl; }
        PIN();
      });
      ca[0] = cat(al[7], ah[7]); ca[1] = cat(al[8], ah[8]); ca[2] = cat(al[9], ah[9]);
      lf = ldsb + (sidx == 2 ? KF_L2 : KF_L0 + sidx * KF_LO); uf = ldsb + KF_UP0 + sidx * KF_UP; sidx = sidx == 2 ? 0 : sidx + 1;
      wait_end<NW>();
    };
#pragma unroll 1
    for (int t = 0; t < 16; t += 2) { gstage(IC<0>{}, IC<7>{}, IC<1>{}, t + 2, bB, bA); gstage(IC<0>{}, IC<6>{}, IC<0>{}, t + 3, bA, bB); }
    gstage(IC<1>{}, IC<5>{}, IC<0>{}, 0, bB, bA);
    gstage(IC<1>{}, IC<10>{}, IC<0>{}, 128, bA, bB);
    bar();
#pragma unroll
    for (int cb = 0; cb < 4; ++cb) acc[7][cb] = MFMA16(ca[0], bB[cb], acc[7][cb]);
#pragma unroll
    for (int cb = 0; cb < 2; ++cb) { acc[8][cb] = MFMA16(ca[1], bB[cb], acc[8][cb]); acc[9][cb] = MFMA16(ca[2], bB[cb], acc[9][cb]); }
    TS(11);
    LAS char* xw0 = smw + (w4 * 4u + (w3 & 1) * 2u) * 9216u + lane * 16u;
    LAS char* xw1 = smw + (w4 * 4u + 2u - (w3 & 1) * 2u) * 9216u + lane * 16u;
#pragma unroll
    for (int u = 0; u < 2; ++u)
#pragma unroll
      for (int cb = 0; cb < 4; ++cb) {
        const f32x4 &ga = acc[4 * u][cb], &ua = acc[4 * u + 1][cb], &gb = acc[4 * u + 2][cb], &ub = acc[4 * u + 3][cb];
        const u32x4 w = {pk2(swiglu(ga[0], ua[0]), swiglu(ga[1], ua[1])), pk2(swiglu(ga[2], ua[2]), swiglu(ga[3], ua[3])),
                         pk2(swiglu(gb[0], ub[0]), swiglu(gb[1], ub[1])), pk2(swiglu(gb[2], ub[2]), swiglu(gb[3], ub[3]))};
        *(LAS u32x4*)((cb < 2 ? xw0 : xw1) + (cb & 1) * 9216 + w3 * 2048u + u * 1024) = w;
      }
#pragma unroll
    for (int cb = 0; cb < 2; ++cb) {
      const f32x4 &ga = acc[8][cb], &ua = acc[9][cb];
      *(LAS u32x2*)(xw0 + cb * 9216 + 8192 + (w3 >> 1) * 8u) = (u32x2){pk2(swiglu(ga[0], ua[0]), swiglu(ga[1], ua[1])), pk2(swiglu(ga[2], ua[2]), swiglu(ga[3], ua[3]))};
    }
    wait_end<5>();
  }
  TS(12);
  u32x4 pgb[2];
  ld16_sc1(pgb[0], vm, sWs, WS_MOD + 5u * 2304u + (wv + 1u) * 256u); ld16_sc1(pgb[1], vm, sWs, WS_MOD + 5u * 2304u);
  {
    const LAS char* pD = sm + KF_L0 + (g >> 1) * 1024u + (w3 >> 1) * 512u + g1 * 256u + q4 * 64u + (((w3 & 1) ^ g1) * 32u) + p4 * 8u;
    const LAS char* const xr = sm + w4 * 36864u + lane * 16u;
    auto dstage = [&](auto mode_, int t, f32x4 (&ac)[4], f32x4 (&accn)[4], u32x2& pvo_, u32x2& pvn) __attribute__((always_inline)) {
      constexpr int MODE = decltype(mode_)::v;
      const unsigned toff = (unsigned)(t + 2) * 128u, lo = ldsb + (sidx == 0 ? 0u : (unsigned)(sidx + 1) * KF_LO);
      const unsigned so = WS_PF + pbase + (unsigned)(t - 1) * 1024u;
      const int dl = sidx == 0 ? KF_LO : (sidx == 1 ? -3 * KF_LO : 2 * KF_LO);
      bf16x8 a[9]; const f32x4 zero = {};
      bar();
      sfor<36>(SL(i)
        prio<i, 36>();
        constexpr int m = (i >> 2) - 2, cb = i & 3;
        if constexpr (m < 0) { if constexpr (MODE != 0) ac[cb] = MFMA16(ca[2 + m], ob[cb][9 + m], ac[cb]); }
        else if constexpr (m == 0) accn[cb] = MFMA16(a[m], ob[cb][m], zero);
        else accn[cb] = MFMA16(a[m], ob[cb][m], accn[cb]);
        PIN();
        constexpr int k = i < 28 ? ((i & 3) == 0 ? i >> 2 : -1) : (i == 28 ? 7 : (i == 30 ? 8 : -1));
        if constexpr (k >= 0) a[k] = rd_tr(pD, k * 4096, k * 4096 + 2048);
        if constexpr (MODE == 0) { if constexpr (i < 28) ob[i & 3][i >> 2] = rd128(xr, (i & 3) * 9216 + (i >> 2) * 1024);
                                   else if constexpr (i < 32) { ob[i & 3][7] = rd128(xr, (i & 3) * 9216 + 7168); ob[i & 3][8] = rd128(xr, (i & 3) * 9216 + 8192); } }
        if constexpr (MODE == 1 && i < 10 && (i & 1)) wd_dma(IC<(i >> 1)>{}, ldsb, 256u);
        constexpr int d0 = MODE == 1 ? 11 : 1;
        constexpr int P = i == d0 ? 0 : (i == d0 + 4 ? 1 : (i == d0 + 8 ? 2 : (i == d0 + 14 ? 3 : (i == d0 + 18 ? 4 : -1))));
        if constexpr ((MODE == 1 || MODE == 2) && P >= 0) wd_dma(IC<P>{}, lo, toff);
        if constexpr (MODE >= 2) side(IC<i>{}, IC<MODE == 2>{}, pvn, t, FG_PF, WS_PF, ldsb + (t >= 7 ? 2u * KF_LO + (unsigned)(t - 7) * 16384u : KF_LO + (t == 5 ? 0u : 16384u)));
        if constexpr (MODE != 0 && i == 14) __builtin_amdgcn_raw_buffer_store_b128(pack44(ac[0], ac[1]), rWs, pvo, so, SC1);
        if constexpr (MODE != 0 && i == 22) __builtin_amdgcn_raw_buffer_store_b128(pack44(ac[2], ac[3]), rWs, pvo, so + 4u * OCT, SC1);
        if constexpr (i == 33) pD += dl;
        PIN();
      });
      ca[0] = a[7]; ca[1] = a[8];
      sidx = sidx == 2 ? 0 : sidx + 1;
      wait_end<MODE == 0 ? 0 : (MODE == 1 ? 7 : (MODE == 2 ? 11 : 5))>();
      if constexpr (MODE >= 2) { if (t < 8) side_end(pvo_, t, FG_PF); }
    };
    sidx = 0; pvA = (u32x2){}; pvB = (u32x2){};
    dstage(IC<0>{}, 0, accB, accA, pvB, pvA);
    asm volatile("" : "+v"(pgb[0]), "+v"(pgb[1]));
    pm[0][0] = gran4(pgb[0], 5, wv + 1u); pm[1][0] = gran4(pgb[1], 5, 0u); asm volatile("" : "+v"(pm[0][0])); asm volatile("" : "+v"(pm[1][0]));
    dstage(IC<1>{}, 1, accA, accB, pvA, pvB);
    dstage(IC<2>{}, 2, accB, accA, pvB, pvA);
#pragma unroll 1
    for (int t = 3; t < 7; t += 2) { dstage(IC<2>{}, t, accA, accB, pvA, pvB); dstage(IC<2>{}, t + 1, accB, accA, pvB, pvA); }
    dstage(IC<3>{}, 7, accA, accB, pvA, pvB);
    dstage(IC<3>{}, 8, accB, accA, pvB, pvA);
#pragma unroll
    for (int i = 0; i < 2; ++i)
#pragma unroll
      for (int cb = 0; cb < 4; ++cb) accA[cb] = MFMA16(ca[i], ob[cb][7 + i], accA[cb]);
  }
  TS(13);
  {
    seam(IC<22>{}, WS_PF, FG_PF, KF_LO, 2 * KF_LO, [&](float (&r)[8], auto z_) __attribute__((always_inline)) {
      constexpr int Z = decltype(z_)::v; const unsigned slab = Z ? 0u : wv + 1u;
      const u32x4 xr = *(const LAS u32x4*)(sm + L_X2 + slab * 1024u + lane * 16u); const f32x4 gv = unp4(pm[Z][0]);
      float y[8];
#pragma unroll
      for (int e = 0; e < 8; ++e) y[e] = ((e & 1) ? bfhi(xr[e >> 1]) : bflo(xr[e >> 1])) + rbf(gv[e & 3] * rbf(r[e]));
      u16* op = out + (size_t)tokA * D + slab * 64u + (lane >> 2) * 4;
      *(u32x2*)op = (u32x2){pk2(y[0], y[1]), pk2(y[2], y[3])};
      *(u32x2*)(op + 16 * D) = (u32x2){pk2(y[4], y[5]), pk2(y[6], y[7])}; });
    TS(14);
  }
}

extern "C" void kernel_launch(void* const* d_in, const int* in_sizes, int n_in, void* d_out, int out_size, void* d_ws, size_t ws_size, hipStream_t stream) {
  (void)hipFuncSetAttribute((const void*)k_block, hipFuncAttributeMaxDynamicSharedMemorySize, 163840);
  k_block<<<256, 512, 163840, stream>>>((const u16*)d_in[0], (const u16*)d_in[1], (const u16*)d_in[2], (const u16*)d_in[3], (const u16*)d_in[4],
                                        (const u16*)d_in[6], (const u16*)d_in[7], (const u16*)d_in[8], (const u16*)d_in[9], (const u16*)d_in[10],
                                        (u16*)d_out, (char*)d_ws);
}
```
